```python
import torch

def _softmax(x, axis=-1):
    return torch.softmax(x, dim=axis)
def _gelu(x):
    return torch.nn.functional.gelu(x, approximate="tanh")
def _silu(x):
    return torch.nn.functional.silu(x)
def _sigmoid(x):
    return torch.sigmoid(x)
def _layernorm(x, axis=-1, eps=1e-5):
    m = x.mean(dim=axis, keepdim=True)
    v = x.var(dim=axis, keepdim=True, unbiased=False)
    return (x - m) / torch.sqrt(v + eps)
def _scatter_add(tgt, idx, src, axis=0):
    return tgt.clone().index_add_(axis, idx, src)
def _segment_sum(src, seg, n):
    out = torch.zeros((n,) + src.shape[1:], dtype=src.dtype)
    return out.index_add_(0, seg, src)
def _pad(x, pw):
    flat = tuple(v for pair in reversed(pw) for v in pair)
    return torch.nn.functional.pad(x, flat)

kernel_name = 'standalone__fused_io_aware_attn__prompt'

def setup_inputs():

    _w = (lambda a: (lambda t: t.to(torch.bfloat16) if t.is_floating_point() else t)(torch.as_tensor(a)))
    B, H, S = 1, 16, 4096
    Dh = max(1, 448 // 16)
    return {
        "q": _w((lambda *s: torch.randn(*s, dtype=torch.bfloat16) * 0.8)(B, H, S, Dh)),
        "k": _w((lambda *s: torch.randn(*s, dtype=torch.bfloat16) * 0.8)(B, H, S, Dh)),
        "v": _w((lambda *s: torch.randn(*s, dtype=torch.bfloat16) * 0.05)(B, H, S, Dh)),
    }

def reference(q, k, v):
    scale = q.shape[-1] ** -0.5
    s = torch.einsum('bhqd,bhkd->bhqk', q, k) * scale
    p = _softmax(s, axis=-1)
    return torch.einsum('bhqk,bhkd->bhqd', p, v)
```

```cpp
#include <hip/hip_runtime.h>
#include <hip/hip_bf16.h>
#include <stdint.h>

typedef float f32x16 __attribute__((ext_vector_type(16)));
typedef short bf16x8 __attribute__((ext_vector_type(8)));
typedef unsigned u32x4 __attribute__((ext_vector_type(4)));
typedef unsigned u32x2 __attribute__((ext_vector_type(2)));

constexpr int H = 16, S = 4096, D = 28;

__device__ __forceinline__ unsigned cvtpk(float lo, float hi) {
  unsigned r; asm("v_cvt_pk_bf16_f32 %0, %1, %2" : "=v"(r) : "v"(lo), "v"(hi)); return r;
}
__device__ __forceinline__ float bflo(unsigned u) { return __uint_as_float(u << 16); }
__device__ __forceinline__ float bfhi(unsigned u) { return __uint_as_float(u & 0xFFFF0000u); }

__device__ __forceinline__ void safe_rows(const uint16_t* __restrict__ Q, const uint16_t* __restrict__ K, const uint16_t* __restrict__ V,
                                          uint16_t* __restrict__ O, int lane) {
  const int r = lane & 31, h = lane >> 5;
  const float C = 0.18898223650461362f * 1.4426950408889634f;
  bf16x8 qf[2];
  {
    const u32x2* qp = (const u32x2*)(Q + r * D + 16 * h);
    u32x2 a = qp[0], b = qp[1], c = qp[2], d = h ? (u32x2){0u, 0u} : qp[3];
    qf[0] = __builtin_bit_cast(bf16x8, (u32x4){a[0], a[1], b[0], b[1]});
    qf[1] = __builtin_bit_cast(bf16x8, (u32x4){c[0], c[1], d[0], d[1]});
  }
  float m = -INFINITY, l = 0.f;
  f32x16 o = {};
  for (int k0 = 0; k0 < S; k0 += 32) {
    const u32x2* kp = (const u32x2*)(K + (k0 + r) * D + 16 * h);
    u32x2 a = kp[0], b = kp[1], c = kp[2], d = h ? (u32x2){0u, 0u} : kp[3];
    bf16x8 k0f = __builtin_bit_cast(bf16x8, (u32x4){a[0], a[1], b[0], b[1]});
    bf16x8 k1f = __builtin_bit_cast(bf16x8, (u32x4){c[0], c[1], d[0], d[1]});
    f32x16 s = {};
    s = __builtin_amdgcn_mfma_f32_32x32x16_bf16(k0f, qf[0], s, 0, 0, 0);
    s = __builtin_amdgcn_mfma_f32_32x32x16_bf16(k1f, qf[1], s, 0, 0, 0);
    float mx = s[0];
#pragma unroll
    for (int i = 1; i < 16; ++i) mx = fmaxf(mx, s[i]);
    mx = fmaxf(mx, __shfl_xor(mx, 32));
    mx *= C;
    const float mn = fmaxf(m, mx);
    const float alpha = (m == -INFINITY) ? 0.f : __builtin_amdgcn_exp2f(m - mn);
    const float mref = (mn == -INFINITY) ? 0.f : mn;
    m = mn;
    float p[16]; float rs = 0.f;
#pragma unroll
    for (int i = 0; i < 16; ++i) { p[i] = __builtin_amdgcn_exp2f(__builtin_fmaf(s[i], C, -mref)); rs += p[i]; }
    rs += __shfl_xor(rs, 32);
    l = l * alpha + rs;
#pragma unroll
    for (int i = 0; i < 16; ++i) o[i] *= alpha;
#pragma unroll
    for (int sk = 0; sk < 2; ++sk) {
      u32x4 pw, vw;
#pragma unroll
      for (int j = 0; j < 4; ++j) pw[j] = cvtpk(p[8 * sk + 2 * j], p[8 * sk + 2 * j + 1]);
#pragma unroll
      for (int j = 0; j < 4; ++j) {
        const int e0 = 2 * j, e1 = 2 * j + 1;
        const int key0 = k0 + 16 * sk + 8 * (e0 >> 2) + 4 * h + (e0 & 3), key1 = k0 + 16 * sk + 8 * (e1 >> 2) + 4 * h + (e1 & 3);
        unsigned lo = r < D ? V[key0 * D + r] : 0u, hi = r < D ? V[key1 * D + r] : 0u;
        vw[j] = lo | (hi << 16);
      }
      o = __builtin_amdgcn_mfma_f32_32x32x16_bf16(__builtin_bit_cast(bf16x8, vw), __builtin_bit_cast(bf16x8, pw), o, 0, 0, 0);
    }
  }
  const float inv = 1.0f / l;
#pragma unroll
  for (int g = 0; g < 4; ++g) {
    const int d0 = 8 * g + 4 * h;
    if (d0 < D) {
      u32x2 w = {cvtpk(o[4 * g] * inv, o[4 * g + 1] * inv), cvtpk(o[4 * g + 2] * inv, o[4 * g + 3] * inv)};
      *(u32x2*)(O + r * D + d0) = w;
    }
  }
}

__global__ __launch_bounds__(256) void attn_base(const uint16_t* __restrict__ q, const uint16_t* __restrict__ k, const uint16_t* __restrict__ v, uint16_t* __restrict__ o) {
  const int wave = blockIdx.x * 4 + (threadIdx.x >> 6), lane = threadIdx.x & 63;
  const int head = wave / (S / 32), q0 = (wave % (S / 32)) * 32;
  const size_t hb = (size_t)head * S * D;
  safe_rows(q + hb + q0 * D, k + hb, v + hb, o + hb + q0 * D, lane);
}

extern "C" void kernel_launch(void* const* d_in, const int* in_sizes, int n_in, void* d_out, int out_size, void* d_ws, size_t ws_size, hipStream_t stream) {
  attn_base<<<H * S / 32 / 4, 256, 0, stream>>>((const uint16_t*)d_in[0], (const uint16_t*)d_in[1], (const uint16_t*)d_in[2], (uint16_t*)d_out);
}
```

```cpp
#include <hip/hip_runtime.h>
#include <hip/hip_bf16.h>
#include <stdint.h>

typedef float f32x16 __attribute__((ext_vector_type(16)));
typedef short bf16x8 __attribute__((ext_vector_type(8)));
typedef unsigned u32x4 __attribute__((ext_vector_type(4)));
typedef unsigned u32x2 __attribute__((ext_vector_type(2)));

constexpr int H = 16, S = 4096, D = 28;
constexpr int NW = 8;
constexpr int LDS_BYTES = 163840, QST_OFF = 8192, RING0 = 36864, RINGB = 8192, XOFF = 36864;
constexpr int KV_REGION = 256 * 56;
constexpr float CSCALE = 0.18898223650461362f * 1.4426950408889634f;

__device__ __forceinline__ unsigned cvtpk(float lo, float hi) {
  unsigned r; asm("v_cvt_pk_bf16_f32 %0, %1, %2" : "=v"(r) : "v"(lo), "v"(hi)); return r;
}

__device__ __forceinline__ void safe_rows(const uint16_t* __restrict__ Q, const uint16_t* __restrict__ K, const uint16_t* __restrict__ V,
                                       uint16_t* __restrict__ O, int lane) {
  const int r = lane & 31, h = lane >> 5;
  bf16x8 qf[2];
  {
    const u32x2* qp = (const u32x2*)(Q + r * D + 16 * h);
    u32x2 a = qp[0], b = qp[1], c = qp[2], d = h ? (u32x2){0u, 0u} : qp[3];
    qf[0] = __builtin_bit_cast(bf16x8, (u32x4){a[0], a[1], b[0], b[1]});
    qf[1] = __builtin_bit_cast(bf16x8, (u32x4){c[0], c[1], d[0], d[1]});
  }
  float m = -INFINITY, l = 0.f;
  f32x16 o = {};
  for (int k0 = 0; k0 < S; k0 += 32) {
    const u32x2* kp = (const u32x2*)(K + (k0 + r) * D + 16 * h);
    u32x2 a = kp[0], b = kp[1], c = kp[2], d = h ? (u32x2){0u, 0u} : kp[3];
    bf16x8 k0f = __builtin_bit_cast(bf16x8, (u32x4){a[0], a[1], b[0], b[1]});
    bf16x8 k1f = __builtin_bit_cast(bf16x8, (u32x4){c[0], c[1], d[0], d[1]});
    f32x16 s = {};
    s = __builtin_amdgcn_mfma_f32_32x32x16_bf16(k0f, qf[0], s, 0, 0, 0);
    s = __builtin_amdgcn_mfma_f32_32x32x16_bf16(k1f, qf[1], s, 0, 0, 0);
    float mx = s[0];
#pragma unroll
    for (int i = 1; i < 16; ++i) mx = fmaxf(mx, s[i]);
    mx = fmaxf(mx, __shfl_xor(mx, 32));
    mx *= CSCALE;
    const float mn = fmaxf(m, mx);
    const float alpha = (m == -INFINITY) ? 0.f : __builtin_amdgcn_exp2f(m - mn);
    const float mref = (mn == -INFINITY) ? 0.f : mn;
    m = mn;
    float p[16]; float rs = 0.f;
#pragma unroll
    for (int i = 0; i < 16; ++i) { p[i] = __builtin_amdgcn_exp2f(__builtin_fmaf(s[i], CSCALE, -mref)); rs += p[i]; }
    rs += __shfl_xor(rs, 32);
    l = l * alpha + rs;
#pragma unroll
    for (int i = 0; i < 16; ++i) o[i] *= alpha;
#pragma unroll
    for (int sk = 0; sk < 2; ++sk) {
      u32x4 pw, vw;
#pragma unroll
      for (int j = 0; j < 4; ++j) pw[j] = cvtpk(p[8 * sk + 2 * j] * 0x1p-12f, p[8 * sk + 2 * j + 1] * 0x1p-12f);
#pragma unroll
      for (int j = 0; j < 4; ++j) {
        const int e0 = 2 * j, e1 = 2 * j + 1;
        const int key0 = k0 + 16 * sk + 8 * (e0 >> 2) + 4 * h + (e0 & 3), key1 = k0 + 16 * sk + 8 * (e1 >> 2) + 4 * h + (e1 & 3);
        unsigned lo = r < D ? V[key0 * D + r] : 0u, hi = r < D ? V[key1 * D + r] : 0u;
        vw[j] = lo | (hi << 16);
      }
      o = __builtin_amdgcn_mfma_f32_32x32x16_bf16(__builtin_bit_cast(bf16x8, vw), __builtin_bit_cast(bf16x8, pw), o, 0, 0, 0);
    }
  }
  const float inv = 4096.0f / l;
#pragma unroll
  for (int g = 0; g < 4; ++g) {
    const int d0 = 8 * g + 4 * h;
    if (d0 < D) {
      u32x2 w = {cvtpk(o[4 * g] * inv, o[4 * g + 1] * inv), cvtpk(o[4 * g + 2] * inv, o[4 * g + 3] * inv)};
      *(u32x2*)(O + r * D + d0) = w;
    }
  }
}

__global__ __launch_bounds__(NW * 64) void attn_fused(const uint16_t* __restrict__ q, const uint16_t* __restrict__ k, const uint16_t* __restrict__ v, uint16_t* __restrict__ out) {
  __shared__ __attribute__((aligned(256))) char lds[LDS_BYTES];
  const int tid = threadIdx.x;
  const int w = __builtin_amdgcn_readfirstlane(tid >> 6);
  const int bid = blockIdx.x;
  const int xcd = bid & 7, j = bid >> 3;
  const int head = xcd * 2 + (j >> 4), qb = j & 15;
  const int qg = w & 1, kq = w >> 1;
  const size_t hb = (size_t)head * S * D;
  const unsigned ldsb = (unsigned)(uintptr_t)lds;
  const bool isv = w >= 4;
  const int pi = w & 3;
  const uint64_t gb = (uint64_t)(isv ? v : k) + hb * 2 + pi * 3584;
  const unsigned glo = (unsigned)gb, ghi = (unsigned)(gb >> 32);
  const unsigned m0b = ldsb + RINGB + (isv ? KV_REGION : 0) + pi * 3584;
  const uint16_t* qp = q + hb + (size_t)(qb * 256 + w * 32) * D;
  const int row0 = qb * 256 + (4 * qg + kq) * 32;
  uint16_t* op = out + hb + (size_t)row0 * D;
  const float cs = CSCALE;
  const unsigned kqoff = ldsb + RING0 + kq * 3584, vvoff = ldsb + RING0 + KV_REGION + kq * 3584, xoff = ldsb + XOFF, qsto = ldsb + QST_OFF + w * 2048, qgo = ldsb + QST_OFF + qg * 8192;
  int flag;
  asm volatile(
      "s_mov_b32 s68, m0\n\t"
      "v_and_b32 v206, 63, %[tid]\n\t"
      "v_and_b32 v207, 31, %[tid]\n\t"
      "v_lshlrev_b32 v203, 4, v206\n\t"
      "v_lshrrev_b32 v208, 5, v206\n\t"
      "v_mul_u32_u24 v207, 56, v207\n\t"
      "v_lshl_add_u32 v205, v208, 5, v207\n\t"
      "v_mad_u32_u24 v209, v208, 24, v207\n\t"
      "global_load_dwordx2 v[8:9], v205, %[qp] offset:0\n\t"
      "global_load_dwordx2 v[10:11], v205, %[qp] offset:8\n\t"
      "global_load_dwordx2 v[12:13], v205, %[qp] offset:16\n\t"
      "global_load_dwordx2 v[14:15], v209, %[qp] offset:24\n\t"
      "s_mov_b32 s61, 0\n\t"
      "s_mov_b32 s62, 28672\n\t"
      "s_add_u32 s66, %[glo], s61\n\t"
      "s_addc_u32 s67, %[ghi], 0\n\t"
      "s_add_u32 s65, %[m0b], s62\n\t"
      "s_mov_b32 m0, s65\n\t"
      "s_nop 0\n\t"
      "global_load_lds_dwordx4 v203, s[66:67] offset:0\n\t"
      "global_load_lds_dwordx4 v203, s[66:67] offset:1024\n\t"
      "global_load_lds_dwordx4 v203, s[66:67] offset:2048\n\t"
      "global_load_lds_dwordx4 v203, s[66:67] offset:2560\n\t"
      "s_add_u32 s61, s61, 14336\n\t"
      "s_cmp_eq_u32 s61, 229376\n\t"
      "s_cselect_b32 s61, 0, s61\n\t"
      "s_add_u32 s62, s62, 28672\n\t"
      "s_cmp_eq_u32 s62, 114688\n\t"
      "s_cselect_b32 s62, 0, s62\n\t"
      "v_add_u32 v200, %[kqoff], v205\n\t"
      "v_lshrrev_b32 v24, 2, v206\n\t"
      "v_and_b32 v25, 4, v24\n\t"
      "v_and_b32 v26, 3, v206\n\t"
      "v_or_b32 v25, v25, v26\n\t"
      "v_and_b32 v24, 3, v24\n\t"
      "v_lshl_add_u32 v24, v208, 3, v24\n\t"
      "v_mul_u32_u24 v24, 56, v24\n\t"
      "v_lshl_add_u32 v24, v25, 3, v24\n\t"
      "v_add_u32 v24, %[vvoff], v24\n\t"
      "v_mul_u32_u24 v26, 448, v208\n\t"
      "v_add_u32 v26, 224, v26\n\t"
      "v_cmp_eq_u32 vcc, 7, v25\n\t"
      "v_mov_b32 v204, 0xdddddddd\n\t"
      "v_lshlrev_b32 v27, 4, %[tid]\n\t"
      "v_cndmask_b32 v201, v24, v26, vcc\n\t"
      "v_cndmask_b32_e64 v202, 1, 0, vcc\n\t"
      "v_mov_b32 v40, 0x3f80\n\t"
      "v_mov_b32 v41, 0\n\t"
      "v_mov_b32 v42, 0x3f80\n\t"
      "v_mov_b32 v43, 0\n\t"
      "ds_write_b128 v27, v[40:43]\n\t"
      "v_mov_b32 v72, 0\n\t"
      "v_mov_b32 v73, 0\n\t"
      "v_mov_b32 v74, 0\n\t"
      "v_mov_b32 v75, 0\n\t"
      "v_mov_b32 v100, 0\n\t"
      "v_mov_b32 v101, 0\n\t"
      "v_mov_b32 v102, 0\n\t"
      "v_mov_b32 v103, 0\n\t"
      "v_mov_b32 v64, 0\n\t"
      "v_mov_b32 v65, 0\n\t"
      "v_mov_b32 v66, 0\n\t"
      "v_mov_b32 v67, 0\n\t"
      "v_mov_b32 v68, 0\n\t"
      "v_mov_b32 v69, 0\n\t"
      "v_mov_b32 v70, 0\n\t"
      "v_mov_b32 v71, 0\n\t"
      "v_mfma_f32_32x32x16_bf16 v[136:151], v[72:75], v[72:75], 0\n\t"
      "v_mfma_f32_32x32x16_bf16 v[152:167], v[72:75], v[72:75], 0\n\t"
      "v_mfma_f32_32x32x16_bf16 v[168:183], v[72:75], v[72:75], 0\n\t"
      "v_mfma_f32_32x32x16_bf16 v[184:199], v[72:75], v[72:75], 0\n\t"
      "v_mul_u32_u24 v214, 24, v208\n\t"
      "v_sub_u32 v214, v205, v214\n\t"
      "v_cmp_eq_u32 vcc, 0, v208\n\t"
      "v_add_u32 v209, %[qsto], v203\n\t"
      "s_add_u32 s65, %[kq], 1\n\t"
      "s_and_b32 s65, s65, 3\n\t"
      "s_lshl_b32 s65, s65, 11\n\t"
      "s_add_u32 s65, s65, %[qgo]\n\t"
      "v_add_u32 v210, s65, v203\n\t"
      "s_add_u32 s65, %[kq], 2\n\t"
      "s_and_b32 s65, s65, 3\n\t"
      "s_lshl_b32 s65, s65, 11\n\t"
      "s_add_u32 s65, s65, %[qgo]\n\t"
      "v_add_u32 v211, s65, v203\n\t"
      "s_add_u32 s65, %[kq], 3\n\t"
      "s_and_b32 s65, s65, 3\n\t"
      "s_lshl_b32 s65, s65, 11\n\t"
      "s_add_u32 s65, s65, %[qgo]\n\t"
      "v_add_u32 v212, s65, v203\n\t"
      "s_add_u32 s65, %[kq], 4\n\t"
      "s_and_b32 s65, s65, 3\n\t"
      "s_lshl_b32 s65, s65, 11\n\t"
      "s_add_u32 s65, s65, %[qgo]\n\t"
      "v_add_u32 v213, s65, v203\n\t"
      "s_mov_b32 s64, 28672\n\t"
      "s_mov_b32 s60, 7\n\t"
      "s_mov_b32 s70, %[cs]\n\t"
      "s_waitcnt vmcnt(4)\n\t"
      "v_lshlrev_b32 v56, 16, v8\n\t"
      "v_and_b32 v57, 0xffff0000, v8\n\t"
      "v_pk_mul_f32 v[56:57], v[56:57], s[70:71] op_sel_hi:[1,0]\n\t"
      "v_cvt_pk_bf16_f32 v32, v56, v57\n\t"
      "v_lshlrev_b32 v58, 16, v9\n\t"
      "v_and_b32 v59, 0xffff0000, v9\n\t"
      "v_pk_mul_f32 v[58:59], v[58:59], s[70:71] op_sel_hi:[1,0]\n\t"
      "v_cvt_pk_bf16_f32 v33, v58, v59\n\t"
      "v_lshlrev_b32 v60, 16, v10\n\t"
      "v_and_b32 v61, 0xffff0000, v10\n\t"
      "v_pk_mul_f32 v[60:61], v[60:61], s[70:71] op_sel_hi:[1,0]\n\t"
      "v_cvt_pk_bf16_f32 v34, v60, v61\n\t"
      "v_lshlrev_b32 v62, 16, v11\n\t"
      "v_and_b32 v63, 0xffff0000, v11\n\t"
      "v_pk_mul_f32 v[62:63], v[62:63], s[70:71] op_sel_hi:[1,0]\n\t"
      "v_cvt_pk_bf16_f32 v35, v62, v63\n\t"
      "ds_write_b128 v209, v[32:35]\n\t"
      "v_lshlrev_b32 v56, 16, v12\n\t"
      "v_and_b32 v57, 0xffff0000, v12\n\t"
      "v_pk_mul_f32 v[56:57], v[56:57], s[70:71] op_sel_hi:[1,0]\n\t"
      "v_cvt_pk_bf16_f32 v36, v56, v57\n\t"
      "v_lshlrev_b32 v58, 16, v13\n\t"
      "v_and_b32 v59, 0xffff0000, v13\n\t"
      "v_pk_mul_f32 v[58:59], v[58:59], s[70:71] op_sel_hi:[1,0]\n\t"
      "v_cvt_pk_bf16_f32 v37, v58, v59\n\t"
      "v_lshlrev_b32 v60, 16, v14\n\t"
      "v_and_b32 v61, 0xffff0000, v14\n\t"
      "v_pk_mul_f32 v[60:61], v[60:61], s[70:71] op_sel_hi:[1,0]\n\t"
      "v_cvt_pk_bf16_f32 v38, v60, v61\n\t"
      "v_lshlrev_b32 v62, 16, v15\n\t"
      "v_and_b32 v63, 0xffff0000, v15\n\t"
      "v_pk_mul_f32 v[62:63], v[62:63], s[70:71] op_sel_hi:[1,0]\n\t"
      "v_cvt_pk_bf16_f32 v39, v62, v63\n\t"
      "v_cndmask_b32 v38, 0, v38, vcc\n\t"
      "v_cndmask_b32 v39, 0, v39, vcc\n\t"
      "ds_write_b128 v209, v[36:39] offset:1024\n\t"
      "s_add_u32 s66, %[glo], s61\n\t"
      "s_addc_u32 s67, %[ghi], 0\n\t"
      "s_add_u32 s65, %[m0b], s62\n\t"
      "s_mov_b32 m0, s65\n\t"
      "s_nop 0\n\t"
      "global_load_lds_dwordx4 v203, s[66:67] offset:0\n\t"
      "global_load_lds_dwordx4 v203, s[66:67] offset:1024\n\t"
      "global_load_lds_dwordx4 v203, s[66:67] offset:2048\n\t"
      "global_load_lds_dwordx4 v203, s[66:67] offset:2560\n\t"
      "s_add_u32 s61, s61, 14336\n\t"
      "s_cmp_eq_u32 s61, 229376\n\t"
      "s_cselect_b32 s61, 0, s61\n\t"
      "s_add_u32 s62, s62, 28672\n\t"
      "s_cmp_eq_u32 s62, 114688\n\t"
      "s_cselect_b32 s62, 0, s62\n\t"
      "s_waitcnt vmcnt(4) lgkmcnt(0)\n\t"
      "s_barrier\n\t"
      "ds_read_b64 v[72:73], v200 offset:0\n\t"
      "ds_read_b64 v[74:75], v200 offset:8\n\t"
      "ds_read_b128 v[104:107], v210\n\t"
      "ds_read_b64 v[80:81], v200 offset:16\n\t"
      "ds_read_b64 v[82:83], v200 offset:24\n\t"
      "ds_read_b128 v[108:111], v210 offset:1024\n\t"
      "ds_read_b128 v[112:115], v211\n\t"
      "ds_read_b128 v[116:119], v211 offset:1024\n\t"
      "ds_read_b128 v[120:123], v212\n\t"
      "ds_read_b128 v[124:127], v212 offset:1024\n\t"
      "ds_read_b128 v[128:131], v213\n\t"
      "ds_read_b128 v[132:135], v213 offset:1024\n\t"
      "ds_read_b64_tr_b16 v[88:89], v201 offset:0\n\t"
      "ds_read_b64_tr_b16 v[90:91], v201 offset:224\n\t"
      "s_waitcnt lgkmcnt(11)\n\t"
      "v_mfma_f32_32x32x16_bf16 v[8:23], v[72:75], v[104:107], 0\n\t"
      "s_waitcnt lgkmcnt(7)\n\t"
      "v_mfma_f32_32x32x16_bf16 v[8:23], v[80:83], v[108:111], v[8:23]\n\t"
      "v_mfma_f32_32x32x16_bf16 v[24:39], v[72:75], v[112:115], 0\n\t"
      "s_add_u32 s66, %[glo], s61\n\t"
      "s_addc_u32 s67, %[ghi], 0\n\t"
      "s_add_u32 s65, %[m0b], s62\n\t"
      "s_mov_b32 m0, s65\n\t"
      "s_nop 0\n\t"
      "global_load_lds_dwordx4 v203, s[66:67] offset:0\n\t"
      "global_load_lds_dwordx4 v203, s[66:67] offset:1024\n\t"
      "global_load_lds_dwordx4 v203, s[66:67] offset:2048\n\t"
      "global_load_lds_dwordx4 v203, s[66:67] offset:2560\n\t"
      "s_add_u32 s61, s61, 14336\n\t"
      "s_cmp_eq_u32 s61, 229376\n\t"
      "s_cselect_b32 s61, 0, s61\n\t"
      "s_add_u32 s62, s62, 28672\n\t"
      "s_cmp_eq_u32 s62, 114688\n\t"
      "s_cselect_b32 s62, 0, s62\n\t"
      "s_waitcnt lgkmcnt(0)\n\t"
      "L_loop_%=:\n\t"
      "v_mfma_f32_32x32x16_bf16 v[24:39], v[80:83], v[116:119], v[24:39]\n\t"
      "ds_read_b64_tr_b16 v[96:97], v201 offset:896\n\t"
      "v_exp_f32 v8, v8\n\t"
      "v_exp_f32 v9, v9\n\t"
      "v_exp_f32 v10, v10\n\t"
      "v_exp_f32 v11, v11\n\t"
      "v_smfmac_f32_32x32x32_bf16 v[184:199], v[100:103], v[64:71], v204\n\t"
      "v_exp_f32 v12, v12\n\t"
      "v_exp_f32 v13, v13\n\t"
      "v_exp_f32 v14, v14\n\t"
      "v_exp_f32 v15, v15\n\t"
      "v_mfma_f32_32x32x16_bf16 v[40:55], v[72:75], v[120:123], 0\n\t"
      "s_waitcnt lgkmcnt(1)\n\t"
      "ds_read_b64_tr_b16 v[98:99], v201 offset:1120\n\t"
      "v_exp_f32 v16, v16\n\t"
      "v_exp_f32 v17, v17\n\t"
      "v_exp_f32 v18, v18\n\t"
      "v_exp_f32 v19, v19\n\t"
      "v_smfmac_f32_32x32x32_bf16 v[136:151], v[88:91], v[8:15], v204\n\t"
      "v_exp_f32 v20, v20\n\t"
      "v_exp_f32 v21, v21\n\t"
      "v_exp_f32 v22, v22\n\t"
      "v_exp_f32 v23, v23\n\t"
      "v_mfma_f32_32x32x16_bf16 v[40:55], v[80:83], v[124:127], v[40:55]\n\t"
      "s_waitcnt lgkmcnt(0)\n\t"
      "ds_read_b64 v[76:77], v200 offset:1792\n\t"
      "v_exp_f32 v24, v24\n\t"
      "v_exp_f32 v25, v25\n\t"
      "v_exp_f32 v26, v26\n\t"
      "v_exp_f32 v27, v27\n\t"
      "v_smfmac_f32_32x32x32_bf16 v[136:151], v[96:99], v[16:23], v204\n\t"
      "v_exp_f32 v28, v28\n\t"
      "v_exp_f32 v29, v29\n\t"
      "v_exp_f32 v30, v30\n\t"
      "v_exp_f32 v31, v31\n\t"
      "v_mfma_f32_32x32x16_bf16 v[56:71], v[72:75], v[128:131], 0\n\t"
      "ds_read_b64 v[78:79], v200 offset:1800\n\t"
      "v_exp_f32 v32, v32\n\t"
      "v_exp_f32 v33, v33\n\t"
      "v_exp_f32 v34, v34\n\t"
      "v_exp_f32 v35, v35\n\t"
      "v_smfmac_f32_32x32x32_bf16 v[152:167], v[88:91], v[24:31], v204\n\t"
      "v_exp_f32 v36, v36\n\t"
      "v_exp_f32 v37, v37\n\t"
      "v_exp_f32 v38, v38\n\t"
      "v_exp_f32 v39, v39\n\t"
      "v_mfma_f32_32x32x16_bf16 v[56:71], v[80:83], v[132:135], v[56:71]\n\t"
      "ds_read_b64 v[84:85], v200 offset:1808\n\t"
      "v_exp_f32 v40, v40\n\t"
      "v_exp_f32 v41, v41\n\t"
      "v_exp_f32 v42, v42\n\t"
      "v_exp_f32 v43, v43\n\t"
      "v_smfmac_f32_32x32x32_bf16 v[152:167], v[96:99], v[32:39], v204\n\t"
      "s_waitcnt lgkmcnt(1)\n\t"
      "v_exp_f32 v44, v44\n\t"
      "v_exp_f32 v45, v45\n\t"
      "v_exp_f32 v46, v46\n\t"
      "v_exp_f32 v47, v47\n\t"
      "s_setprio 0\n\t"
      "v_mfma_f32_32x32x16_bf16 v[8:23], v[76:79], v[104:107], 0\n\t"
      "ds_read_b64 v[86:87], v200 offset:1816\n\t"
      "v_exp_f32 v48, v48\n\t"
      "v_exp_f32 v49, v49\n\t"
      "v_exp_f32 v50, v50\n\t"
      "v_exp_f32 v51, v51\n\t"
      "v_smfmac_f32_32x32x32_bf16 v[168:183], v[88:91], v[40:47], v204\n\t"
      "s_waitcnt lgkmcnt(0)\n\t"
      "v_exp_f32 v52, v52\n\t"
      "v_exp_f32 v53, v53\n\t"
      "v_exp_f32 v54, v54\n\t"
      "v_exp_f32 v55, v55\n\t"
      "v_mfma_f32_32x32x16_bf16 v[8:23], v[84:87], v[108:111], v[8:23]\n\t"
      "ds_read_b64_tr_b16 v[92:93], v201 offset:1792\n\t"
      "v_exp_f32 v56, v56\n\t"
      "v_exp_f32 v57, v57\n\t"
      "v_exp_f32 v58, v58\n\t"
      "v_exp_f32 v59, v59\n\t"
      "v_smfmac_f32_32x32x32_bf16 v[168:183], v[96:99], v[48:55], v204\n\t"
      "v_exp_f32 v60, v60\n\t"
      "v_exp_f32 v61, v61\n\t"
      "v_exp_f32 v62, v62\n\t"
      "v_exp_f32 v63, v63\n\t"
      "v_mfma_f32_32x32x16_bf16 v[24:39], v[76:79], v[112:115], 0\n\t"
      "ds_read_b64_tr_b16 v[94:95], v201 offset:2016\n\t"
      "v_exp_f32 v64, v64\n\t"
      "v_exp_f32 v65, v65\n\t"
      "v_exp_f32 v66, v66\n\t"
      "v_exp_f32 v67, v67\n\t"
      "v_smfmac_f32_32x32x32_bf16 v[184:199], v[88:91], v[56:63], v204\n\t"
      "v_exp_f32 v68, v68\n\t"
      "v_exp_f32 v69, v69\n\t"
      "v_exp_f32 v70, v70\n\t"
      "v_exp_f32 v71, v71\n\t"
      "v_mfma_f32_32x32x16_bf16 v[24:39], v[84:87], v[116:119], v[24:39]\n\t"
      "ds_read_b64_tr_b16 v[100:101], v201 offset:2688\n\t"
      "v_exp_f32 v8, v8\n\t"
      "v_exp_f32 v9, v9\n\t"
      "v_exp_f32 v10, v10\n\t"
      "v_exp_f32 v11, v11\n\t"
      "v_smfmac_f32_32x32x32_bf16 v[184:199], v[96:99], v[64:71], v204\n\t"
      "v_exp_f32 v12, v12\n\t"
      "v_exp_f32 v13, v13\n\t"
      "v_exp_f32 v14, v14\n\t"
      "v_exp_f32 v15, v15\n\t"
      "v_mfma_f32_32x32x16_bf16 v[40:55], v[76:79], v[120:123], 0\n\t"
      "s_waitcnt lgkmcnt(1)\n\t"
      "ds_read_b64_tr_b16 v[102:103], v201 offset:2912\n\t"
      "v_exp_f32 v16, v16\n\t"
      "v_exp_f32 v17, v17\n\t"
      "v_exp_f32 v18, v18\n\t"
      "v_exp_f32 v19, v19\n\t"
      "v_smfmac_f32_32x32x32_bf16 v[136:151], v[92:95], v[8:15], v204\n\t"
      "v_exp_f32 v20, v20\n\t"
      "v_exp_f32 v21, v21\n\t"
      "v_exp_f32 v22, v22\n\t"
      "v_exp_f32 v23, v23\n\t"
      "s_waitcnt vmcnt(0) lgkmcnt(0)\n\t"
      "s_barrier\n\t"
      "s_cmp_ge_u32 s60, 2\n\t"
      "s_cselect_b64 s[70:71], -1, 0\n\t"
      "s_mov_b32 s63, s64\n\t"
      "s_add_u32 s64, s64, 28672\n\t"
      "s_cmp_eq_u32 s64, 114688\n\t"
      "s_cselect_b32 s64, 0, s64\n\t"
      "s_sub_u32 s63, s64, s63\n\t"
      "v_add_u32 v200, s63, v200\n\t"
      "s_setprio 3\n\t"
      "v_mfma_f32_32x32x16_bf16 v[40:55], v[84:87], v[124:127], v[40:55]\n\t"
      "s_waitcnt lgkmcnt(0)\n\t"
      "ds_read_b64 v[72:73], v200 offset:0\n\t"
      "v_exp_f32 v24, v24\n\t"
      "v_exp_f32 v25, v25\n\t"
      "v_exp_f32 v26, v26\n\t"
      "v_exp_f32 v27, v27\n\t"
      "v_smfmac_f32_32x32x32_bf16 v[136:151], v[100:103], v[16:23], v204\n\t"
      "v_exp_f32 v28, v28\n\t"
      "v_exp_f32 v29, v29\n\t"
      "v_exp_f32 v30, v30\n\t"
      "v_exp_f32 v31, v31\n\t"
      "s_add_u32 s66, %[glo], s61\n\t"
      "s_addc_u32 s67, %[ghi], 0\n\t"
      "s_add_u32 s65, %[m0b], s62\n\t"
      "s_mov_b32 m0, s65\n\t"
      "s_nop 0\n\t"
      "global_load_lds_dwordx4 v203, s[66:67] offset:0\n\t"
      "v_mfma_f32_32x32x16_bf16 v[56:71], v[76:79], v[128:131], 0\n\t"
      "ds_read_b64 v[74:75], v200 offset:8\n\t"
      "v_exp_f32 v32, v32\n\t"
      "v_exp_f32 v33, v33\n\t"
      "v_exp_f32 v34, v34\n\t"
      "v_exp_f32 v35, v35\n\t"
      "v_smfmac_f32_32x32x32_bf16 v[152:167], v[92:95], v[24:31], v204\n\t"
      "v_exp_f32 v36, v36\n\t"
      "v_exp_f32 v37, v37\n\t"
      "v_exp_f32 v38, v38\n\t"
      "v_exp_f32 v39, v39\n\t"
      "global_load_lds_dwordx4 v203, s[66:67] offset:1024\n\t"
      "v_mfma_f32_32x32x16_bf16 v[56:71], v[84:87], v[132:135], v[56:71]\n\t"
      "ds_read_b64 v[80:81], v200 offset:16\n\t"
      "v_exp_f32 v40, v40\n\t"
      "v_exp_f32 v41, v41\n\t"
      "v_exp_f32 v42, v42\n\t"
      "v_exp_f32 v43, v43\n\t"
      "v_smfmac_f32_32x32x32_bf16 v[152:167], v[100:103], v[32:39], v204\n\t"
      "s_waitcnt lgkmcnt(1)\n\t"
      "v_exp_f32 v44, v44\n\t"
      "v_exp_f32 v45, v45\n\t"
      "v_exp_f32 v46, v46\n\t"
      "v_exp_f32 v47, v47\n\t"
      "global_load_lds_dwordx4 v203, s[66:67] offset:2048\n\t"
      "v_mfma_f32_32x32x16_bf16 v[8:23], v[72:75], v[104:107], 0\n\t"
      "ds_read_b64 v[82:83], v200 offset:24\n\t"
      "v_exp_f32 v48, v48\n\t"
      "v_exp_f32 v49, v49\n\t"
      "v_exp_f32 v50, v50\n\t"
      "v_exp_f32 v51, v51\n\t"
      "v_smfmac_f32_32x32x32_bf16 v[168:183], v[92:95], v[40:47], v204\n\t"
      "s_waitcnt lgkmcnt(0)\n\t"
      "v_exp_f32 v52, v52\n\t"
      "v_exp_f32 v53, v53\n\t"
      "v_exp_f32 v54, v54\n\t"
      "v_exp_f32 v55, v55\n\t"
      "global_load_lds_dwordx4 v203, s[66:67] offset:2560\n\t"
      "s_add_u32 s61, s61, 14336\n\t"
      "s_cmp_eq_u32 s61, 229376\n\t"
      "s_cselect_b32 s61, 0, s61\n\t"
      "s_add_u32 s62, s62, 28672\n\t"
      "s_cmp_eq_u32 s62, 114688\n\t"
      "s_cselect_b32 s62, 0, s62\n\t"
      "v_mad_i32_i24 v201, v202, s63, v201\n\t"
      "v_mfma_f32_32x32x16_bf16 v[8:23], v[80:83], v[108:111], v[8:23]\n\t"
      "ds_read_b64_tr_b16 v[88:89], v201 offset:0\n\t"
      "v_exp_f32 v56, v56\n\t"
      "v_exp_f32 v57, v57\n\t"
      "v_exp_f32 v58, v58\n\t"
      "v_exp_f32 v59, v59\n\t"
      "v_smfmac_f32_32x32x32_bf16 v[168:183], v[100:103], v[48:55], v204\n\t"
      "v_exp_f32 v60, v60\n\t"
      "v_exp_f32 v61, v61\n\t"
      "v_exp_f32 v62, v62\n\t"
      "v_exp_f32 v63, v63\n\t"
      "s_add_u32 s66, %[glo], s61\n\t"
      "s_addc_u32 s67, %[ghi], 0\n\t"
      "s_add_u32 s65, %[m0b], s62\n\t"
      "s_mov_b32 m0, s65\n\t"
      "s_mov_b64 exec, s[70:71]\n\t"
      "global_load_lds_dwordx4 v203, s[66:67] offset:0\n\t"
      "s_mov_b64 exec, -1\n\t"
      "v_mfma_f32_32x32x16_bf16 v[24:39], v[72:75], v[112:115], 0\n\t"
      "ds_read_b64_tr_b16 v[90:91], v201 offset:224\n\t"
      "v_exp_f32 v64, v64\n\t"
      "v_exp_f32 v65, v65\n\t"
      "v_exp_f32 v66, v66\n\t"
      "v_exp_f32 v67, v67\n\t"
      "v_smfmac_f32_32x32x32_bf16 v[184:199], v[92:95], v[56:63], v204\n\t"
      "v_exp_f32 v68, v68\n\t"
      "v_exp_f32 v69, v69\n\t"
      "v_exp_f32 v70, v70\n\t"
      "v_exp_f32 v71, v71\n\t"
      "s_mov_b64 exec, s[70:71]\n\t"
      "global_load_lds_dwordx4 v203, s[66:67] offset:1024\n\t"
      "s_mov_b64 exec, -1\n\t"
      "v_mfma_f32_32x32x16_bf16 v[24:39], v[80:83], v[116:119], v[24:39]\n\t"
      "ds_read_b64_tr_b16 v[96:97], v201 offset:896\n\t"
      "v_exp_f32 v8, v8\n\t"
      "v_exp_f32 v9, v9\n\t"
      "v_exp_f32 v10, v10\n\t"
      "v_exp_f32 v11, v11\n\t"
      "v_smfmac_f32_32x32x32_bf16 v[184:199], v[100:103], v[64:71], v204\n\t"
      "v_exp_f32 v12, v12\n\t"
      "v_exp_f32 v13, v13\n\t"
      "v_exp_f32 v14, v14\n\t"
      "v_exp_f32 v15, v15\n\t"
      "s_mov_b64 exec, s[70:71]\n\t"
      "global_load_lds_dwordx4 v203, s[66:67] offset:2048\n\t"
      "s_mov_b64 exec, -1\n\t"
      "v_mfma_f32_32x32x16_bf16 v[40:55], v[72:75], v[120:123], 0\n\t"
      "s_waitcnt lgkmcnt(1)\n\t"
      "ds_read_b64_tr_b16 v[98:99], v201 offset:1120\n\t"
      "v_exp_f32 v16, v16\n\t"
      "v_exp_f32 v17, v17\n\t"
      "v_exp_f32 v18, v18\n\t"
      "v_exp_f32 v19, v19\n\t"
      "v_smfmac_f32_32x32x32_bf16 v[136:151], v[88:91], v[8:15], v204\n\t"
      "v_exp_f32 v20, v20\n\t"
      "v_exp_f32 v21, v21\n\t"
      "v_exp_f32 v22, v22\n\t"
      "v_exp_f32 v23, v23\n\t"
      "s_mov_b64 exec, s[70:71]\n\t"
      "global_load_lds_dwordx4 v203, s[66:67] offset:2560\n\t"
      "s_mov_b64 exec, -1\n\t"
      "s_add_u32 s61, s61, 14336\n\t"
      "s_cmp_eq_u32 s61, 229376\n\t"
      "s_cselect_b32 s61, 0, s61\n\t"
      "s_add_u32 s62, s62, 28672\n\t"
      "s_cmp_eq_u32 s62, 114688\n\t"
      "s_cselect_b32 s62, 0, s62\n\t"
      "v_mfma_f32_32x32x16_bf16 v[40:55], v[80:83], v[124:127], v[40:55]\n\t"
      "s_waitcnt lgkmcnt(0)\n\t"
      "ds_read_b64 v[76:77], v200 offset:1792\n\t"
      "v_exp_f32 v24, v24\n\t"
      "v_exp_f32 v25, v25\n\t"
      "v_exp_f32 v26, v26\n\t"
      "v_exp_f32 v27, v27\n\t"
      "v_smfmac_f32_32x32x32_bf16 v[136:151], v[96:99], v[16:23], v204\n\t"
      "v_exp_f32 v28, v28\n\t"
      "v_exp_f32 v29, v29\n\t"
      "v_exp_f32 v30, v30\n\t"
      "v_exp_f32 v31, v31\n\t"
      "v_mfma_f32_32x32x16_bf16 v[56:71], v[72:75], v[128:131], 0\n\t"
      "ds_read_b64 v[78:79], v200 offset:1800\n\t"
      "v_exp_f32 v32, v32\n\t"
      "v_exp_f32 v33, v33\n\t"
      "v_exp_f32 v34, v34\n\t"
      "v_exp_f32 v35, v35\n\t"
      "v_smfmac_f32_32x32x32_bf16 v[152:167], v[88:91], v[24:31], v204\n\t"
      "v_exp_f32 v36, v36\n\t"
      "v_exp_f32 v37, v37\n\t"
      "v_exp_f32 v38, v38\n\t"
      "v_exp_f32 v39, v39\n\t"
      "v_mfma_f32_32x32x16_bf16 v[56:71], v[80:83], v[132:135], v[56:71]\n\t"
      "ds_read_b64 v[84:85], v200 offset:1808\n\t"
      "v_exp_f32 v40, v40\n\t"
      "v_exp_f32 v41, v41\n\t"
      "v_exp_f32 v42, v42\n\t"
      "v_exp_f32 v43, v43\n\t"
      "v_smfmac_f32_32x32x32_bf16 v[152:167], v[96:99], v[32:39], v204\n\t"
      "s_waitcnt lgkmcnt(1)\n\t"
      "v_exp_f32 v44, v44\n\t"
      "v_exp_f32 v45, v45\n\t"
      "v_exp_f32 v46, v46\n\t"
      "v_exp_f32 v47, v47\n\t"
      "v_mfma_f32_32x32x16_bf16 v[8:23], v[76:79], v[104:107], 0\n\t"
      "ds_read_b64 v[86:87], v200 offset:1816\n\t"
      "v_exp_f32 v48, v48\n\t"
      "v_exp_f32 v49, v49\n\t"
      "v_exp_f32 v50, v50\n\t"
      "v_exp_f32 v51, v51\n\t"
      "v_smfmac_f32_32x32x32_bf16 v[168:183], v[88:91], v[40:47], v204\n\t"
      "s_waitcnt lgkmcnt(0)\n\t"
      "v_exp_f32 v52, v52\n\t"
      "v_exp_f32 v53, v53\n\t"
      "v_exp_f32 v54, v54\n\t"
      "v_exp_f32 v55, v55\n\t"
      "s_setprio 2\n\t"
      "v_mfma_f32_32x32x16_bf16 v[8:23], v[84:87], v[108:111], v[8:23]\n\t"
      "ds_read_b64_tr_b16 v[92:93], v201 offset:1792\n\t"
      "v_exp_f32 v56, v56\n\t"
      "v_exp_f32 v57, v57\n\t"
      "v_exp_f32 v58, v58\n\t"
      "v_exp_f32 v59, v59\n\t"
      "v_smfmac_f32_32x32x32_bf16 v[168:183], v[96:99], v[48:55], v204\n\t"
      "v_exp_f32 v60, v60\n\t"
      "v_exp_f32 v61, v61\n\t"
      "v_exp_f32 v62, v62\n\t"
      "v_exp_f32 v63, v63\n\t"
      "v_mfma_f32_32x32x16_bf16 v[24:39], v[76:79], v[112:115], 0\n\t"
      "ds_read_b64_tr_b16 v[94:95], v201 offset:2016\n\t"
      "v_exp_f32 v64, v64\n\t"
      "v_exp_f32 v65, v65\n\t"
      "v_exp_f32 v66, v66\n\t"
      "v_exp_f32 v67, v67\n\t"
      "v_smfmac_f32_32x32x32_bf16 v[184:199], v[88:91], v[56:63], v204\n\t"
      "v_exp_f32 v68, v68\n\t"
      "v_exp_f32 v69, v69\n\t"
      "v_exp_f32 v70, v70\n\t"
      "v_exp_f32 v71, v71\n\t"
      "v_mfma_f32_32x32x16_bf16 v[24:39], v[84:87], v[116:119], v[24:39]\n\t"
      "ds_read_b64_tr_b16 v[100:101], v201 offset:2688\n\t"
      "v_exp_f32 v8, v8\n\t"
      "v_exp_f32 v9, v9\n\t"
      "v_exp_f32 v10, v10\n\t"
      "v_exp_f32 v11, v11\n\t"
      "v_smfmac_f32_32x32x32_bf16 v[184:199], v[96:99], v[64:71], v204\n\t"
      "v_exp_f32 v12, v12\n\t"
      "v_exp_f32 v13, v13\n\t"
      "v_exp_f32 v14, v14\n\t"
      "v_exp_f32 v15, v15\n\t"
      "v_mfma_f32_32x32x16_bf16 v[40:55], v[76:79], v[120:123], 0\n\t"
      "s_waitcnt lgkmcnt(1)\n\t"
      "ds_read_b64_tr_b16 v[102:103], v201 offset:2912\n\t"
      "v_exp_f32 v16, v16\n\t"
      "v_exp_f32 v17, v17\n\t"
      "v_exp_f32 v18, v18\n\t"
      "v_exp_f32 v19, v19\n\t"
      "v_smfmac_f32_32x32x32_bf16 v[136:151], v[92:95], v[8:15], v204\n\t"
      "v_exp_f32 v20, v20\n\t"
      "v_exp_f32 v21, v21\n\t"
      "v_exp_f32 v22, v22\n\t"
      "v_exp_f32 v23, v23\n\t"
      "s_mov_b32 s63, s64\n\t"
      "s_add_u32 s64, s64, 28672\n\t"
      "s_cmp_eq_u32 s64, 114688\n\t"
      "s_cselect_b32 s64, 0, s64\n\t"
      "s_sub_u32 s63, s64, s63\n\t"
      "v_add_u32 v200, s63, v200\n\t"
      "v_mfma_f32_32x32x16_bf16 v[40:55], v[84:87], v[124:127], v[40:55]\n\t"
      "s_waitcnt lgkmcnt(0)\n\t"
      "ds_read_b64 v[72:73], v200 offset:0\n\t"
      "v_exp_f32 v24, v24\n\t"
      "v_exp_f32 v25, v25\n\t"
      "v_exp_f32 v26, v26\n\t"
      "v_exp_f32 v27, v27\n\t"
      "v_smfmac_f32_32x32x32_bf16 v[136:151], v[100:103], v[16:23], v204\n\t"
      "v_exp_f32 v28, v28\n\t"
      "v_exp_f32 v29, v29\n\t"
      "v_exp_f32 v30, v30\n\t"
      "v_exp_f32 v31, v31\n\t"
      "v_mfma_f32_32x32x16_bf16 v[56:71], v[76:79], v[128:131], 0\n\t"
      "ds_read_b64 v[74:75], v200 offset:8\n\t"
      "v_exp_f32 v32, v32\n\t"
      "v_exp_f32 v33, v33\n\t"
      "v_exp_f32 v34, v34\n\t"
      "v_exp_f32 v35, v35\n\t"
      "v_smfmac_f32_32x32x32_bf16 v[152:167], v[92:95], v[24:31], v204\n\t"
      "v_exp_f32 v36, v36\n\t"
      "v_exp_f32 v37, v37\n\t"
      "v_exp_f32 v38, v38\n\t"
      "v_exp_f32 v39, v39\n\t"
      "v_mfma_f32_32x32x16_bf16 v[56:71], v[84:87], v[132:135], v[56:71]\n\t"
      "ds_read_b64 v[80:81], v200 offset:16\n\t"
      "v_exp_f32 v40, v40\n\t"
      "v_exp_f32 v41, v41\n\t"
      "v_exp_f32 v42, v42\n\t"
      "v_exp_f32 v43, v43\n\t"
      "v_smfmac_f32_32x32x32_bf16 v[152:167], v[100:103], v[32:39], v204\n\t"
      "s_waitcnt lgkmcnt(1)\n\t"
      "v_exp_f32 v44, v44\n\t"
      "v_exp_f32 v45, v45\n\t"
      "v_exp_f32 v46, v46\n\t"
      "v_exp_f32 v47, v47\n\t"
      "v_mfma_f32_32x32x16_bf16 v[8:23], v[72:75], v[104:107], 0\n\t"
      "ds_read_b64 v[82:83], v200 offset:24\n\t"
      "v_exp_f32 v48, v48\n\t"
      "v_exp_f32 v49, v49\n\t"
      "v_exp_f32 v50, v50\n\t"
      "v_exp_f32 v51, v51\n\t"
      "v_smfmac_f32_32x32x32_bf16 v[168:183], v[92:95], v[40:47], v204\n\t"
      "s_waitcnt lgkmcnt(0)\n\t"
      "v_exp_f32 v52, v52\n\t"
      "v_exp_f32 v53, v53\n\t"
      "v_exp_f32 v54, v54\n\t"
      "v_exp_f32 v55, v55\n\t"
      "v_mad_i32_i24 v201, v202, s63, v201\n\t"
      "s_setprio 1\n\t"
      "v_mfma_f32_32x32x16_bf16 v[8:23], v[80:83], v[108:111], v[8:23]\n\t"
      "ds_read_b64_tr_b16 v[88:89], v201 offset:0\n\t"
      "v_exp_f32 v56, v56\n\t"
      "v_exp_f32 v57, v57\n\t"
      "v_exp_f32 v58, v58\n\t"
      "v_exp_f32 v59, v59\n\t"
      "v_smfmac_f32_32x32x32_bf16 v[168:183], v[100:103], v[48:55], v204\n\t"
      "v_exp_f32 v60, v60\n\t"
      "v_exp_f32 v61, v61\n\t"
      "v_exp_f32 v62, v62\n\t"
      "v_exp_f32 v63, v63\n\t"
      "v_mfma_f32_32x32x16_bf16 v[24:39], v[72:75], v[112:115], 0\n\t"
      "ds_read_b64_tr_b16 v[90:91], v201 offset:224\n\t"
      "v_exp_f32 v64, v64\n\t"
      "v_exp_f32 v65, v65\n\t"
      "v_exp_f32 v66, v66\n\t"
      "v_exp_f32 v67, v67\n\t"
      "v_smfmac_f32_32x32x32_bf16 v[184:199], v[92:95], v[56:63], v204\n\t"
      "v_exp_f32 v68, v68\n\t"
      "v_exp_f32 v69, v69\n\t"
      "v_exp_f32 v70, v70\n\t"
      "v_exp_f32 v71, v71\n\t"
      "s_sub_u32 s60, s60, 1\n\t"
      "s_cmp_lg_u32 s60, 0\n\t"
      "s_cbranch_scc1 L_loop_%=\n\t"
      "s_setprio 1\n\t"
      "v_mfma_f32_32x32x16_bf16 v[24:39], v[80:83], v[116:119], v[24:39]\n\t"
      "ds_read_b64_tr_b16 v[96:97], v201 offset:896\n\t"
      "v_exp_f32 v8, v8\n\t"
      "v_exp_f32 v9, v9\n\t"
      "v_exp_f32 v10, v10\n\t"
      "v_exp_f32 v11, v11\n\t"
      "v_add_u32 v215, 4294881280, v200\n\t"
      "s_mov_b32 s63, 4294881280\n\t"
      "v_smfmac_f32_32x32x32_bf16 v[184:199], v[100:103], v[64:71], v204\n\t"
      "ds_read_b64_tr_b16 v[98:99], v201 offset:1120\n\t"
      "v_exp_f32 v12, v12\n\t"
      "v_exp_f32 v13, v13\n\t"
      "v_exp_f32 v14, v14\n\t"
      "v_exp_f32 v15, v15\n\t"
      "v_mov_b32 v216, v201\n\t"
      "v_mad_i32_i24 v216, v202, s63, v216\n\t"
      "v_mfma_f32_32x32x16_bf16 v[40:55], v[72:75], v[120:123], 0\n\t"
      "s_waitcnt lgkmcnt(2)\n\t"
      "ds_read_b64 v[76:77], v200 offset:1792\n\t"
      "v_exp_f32 v16, v16\n\t"
      "v_exp_f32 v17, v17\n\t"
      "v_exp_f32 v18, v18\n\t"
      "v_exp_f32 v19, v19\n\t"
      "s_add_u32 s65, %[kq], 1\n\t"
      "s_and_b32 s65, s65, 3\n\t"
      "s_lshl_b32 s65, s65, 1\n\t"
      "s_add_u32 s65, s65, %[qg]\n\t"
      "s_mul_i32 s65, s65, 3\n\t"
      "s_add_u32 s65, s65, 0\n\t"
      "s_cmp_lt_u32 s65, 14\n\t"
      "s_cselect_b32 s69, 0, 28672\n\t"
      "s_lshl_b32 s66, s65, 12\n\t"
      "s_add_u32 s66, s66, s69\n\t"
      "s_add_u32 s66, s66, %[xoff]\n\t"
      "v_add_u32 v210, s66, v203\n\t"
      "v_smfmac_f32_32x32x32_bf16 v[136:151], v[88:91], v[8:15], v204\n\t"
      "ds_read_b64 v[78:79], v200 offset:1800\n\t"
      "v_exp_f32 v20, v20\n\t"
      "v_exp_f32 v21, v21\n\t"
      "v_exp_f32 v22, v22\n\t"
      "v_exp_f32 v23, v23\n\t"
      "s_add_u32 s65, %[kq], 2\n\t"
      "s_and_b32 s65, s65, 3\n\t"
      "s_lshl_b32 s65, s65, 1\n\t"
      "s_add_u32 s65, s65, %[qg]\n\t"
      "s_mul_i32 s65, s65, 3\n\t"
      "s_add_u32 s65, s65, 1\n\t"
      "s_cmp_lt_u32 s65, 14\n\t"
      "s_cselect_b32 s69, 0, 28672\n\t"
      "s_lshl_b32 s66, s65, 12\n\t"
      "s_add_u32 s66, s66, s69\n\t"
      "s_add_u32 s66, s66, %[xoff]\n\t"
      "v_add_u32 v211, s66, v203\n\t"
      "v_mfma_f32_32x32x16_bf16 v[40:55], v[80:83], v[124:127], v[40:55]\n\t"
      "s_waitcnt lgkmcnt(2)\n\t"
      "ds_read_b64 v[84:85], v200 offset:1808\n\t"
      "v_exp_f32 v24, v24\n\t"
      "v_exp_f32 v25, v25\n\t"
      "v_exp_f32 v26, v26\n\t"
      "v_exp_f32 v27, v27\n\t"
      "s_add_u32 s65, %[kq], 3\n\t"
      "s_and_b32 s65, s65, 3\n\t"
      "s_lshl_b32 s65, s65, 1\n\t"
      "s_add_u32 s65, s65, %[qg]\n\t"
      "s_mul_i32 s65, s65, 3\n\t"
      "s_add_u32 s65, s65, 2\n\t"
      "s_cmp_lt_u32 s65, 14\n\t"
      "s_cselect_b32 s69, 0, 28672\n\t"
      "s_lshl_b32 s66, s65, 12\n\t"
      "s_add_u32 s66, s66, s69\n\t"
      "s_add_u32 s66, s66, %[xoff]\n\t"
      "v_add_u32 v212, s66, v203\n\t"
      "v_smfmac_f32_32x32x32_bf16 v[136:151], v[96:99], v[16:23], v204\n\t"
      "s_waitcnt lgkmcnt(1)\n\t"
      "ds_read_b64 v[86:87], v200 offset:1816\n\t"
      "v_exp_f32 v28, v28\n\t"
      "v_exp_f32 v29, v29\n\t"
      "v_exp_f32 v30, v30\n\t"
      "v_exp_f32 v31, v31\n\t"
      "s_mul_i32 s65, %[w], 3\n\t"
      "s_add_u32 s65, s65, 0\n\t"
      "s_cmp_lt_u32 s65, 14\n\t"
      "s_cselect_b32 s69, 0, 28672\n\t"
      "s_lshl_b32 s66, s65, 12\n\t"
      "s_add_u32 s66, s66, s69\n\t"
      "s_add_u32 s66, s66, %[xoff]\n\t"
      "v_add_u32 v206, s66, v203\n\t"
      "v_add_u32 v209, s66, v214\n\t"
      "v_mfma_f32_32x32x16_bf16 v[56:71], v[76:79], v[104:107], 0\n\t"
      "v_exp_f32 v32, v32\n\t"
      "v_exp_f32 v33, v33\n\t"
      "v_exp_f32 v34, v34\n\t"
      "v_exp_f32 v35, v35\n\t"
      "s_mul_i32 s65, %[w], 3\n\t"
      "s_add_u32 s65, s65, 1\n\t"
      "s_cmp_lt_u32 s65, 14\n\t"
      "s_cselect_b32 s69, 0, 28672\n\t"
      "s_lshl_b32 s66, s65, 12\n\t"
      "s_add_u32 s66, s66, s69\n\t"
      "s_add_u32 s66, s66, %[xoff]\n\t"
      "v_add_u32 v207, s66, v203\n\t"
      "v_smfmac_f32_32x32x32_bf16 v[152:167], v[88:91], v[24:31], v204\n\t"
      "s_waitcnt lgkmcnt(0)\n\t"
      "ds_read_b64_tr_b16 v[92:93], v201 offset:1792\n\t"
      "v_exp_f32 v36, v36\n\t"
      "v_exp_f32 v37, v37\n\t"
      "v_exp_f32 v38, v38\n\t"
      "v_exp_f32 v39, v39\n\t"
      "s_mul_i32 s65, %[w], 3\n\t"
      "s_add_u32 s65, s65, 2\n\t"
      "s_cmp_lt_u32 s65, 14\n\t"
      "s_cselect_b32 s69, 0, 28672\n\t"
      "s_lshl_b32 s66, s65, 12\n\t"
      "s_add_u32 s66, s66, s69\n\t"
      "s_add_u32 s66, s66, %[xoff]\n\t"
      "v_add_u32 v208, s66, v203\n\t"
      "s_waitcnt vmcnt(0) lgkmcnt(0)\n\t"
      "s_barrier\n\t"
      "v_mfma_f32_32x32x16_bf16 v[56:71], v[84:87], v[108:111], v[56:71]\n\t"
      "ds_read_b64_tr_b16 v[94:95], v201 offset:2016\n\t"
      "v_exp_f32 v40, v40\n\t"
      "v_exp_f32 v41, v41\n\t"
      "v_exp_f32 v42, v42\n\t"
      "v_exp_f32 v43, v43\n\t"
      "v_smfmac_f32_32x32x32_bf16 v[152:167], v[96:99], v[32:39], v204\n\t"
      "ds_read_b64_tr_b16 v[100:101], v201 offset:2688\n\t"
      "v_exp_f32 v44, v44\n\t"
      "v_exp_f32 v45, v45\n\t"
      "v_exp_f32 v46, v46\n\t"
      "v_exp_f32 v47, v47\n\t"
      "v_mfma_f32_32x32x16_bf16 v[8:23], v[76:79], v[112:115], 0\n\t"
      "ds_read_b64_tr_b16 v[102:103], v201 offset:2912\n\t"
      "v_exp_f32 v48, v48\n\t"
      "v_exp_f32 v49, v49\n\t"
      "v_exp_f32 v50, v50\n\t"
      "v_exp_f32 v51, v51\n\t"
      "v_smfmac_f32_32x32x32_bf16 v[168:183], v[88:91], v[40:47], v204\n\t"
      "ds_read_b64 v[72:73], v215 offset:0\n\t"
      "v_exp_f32 v52, v52\n\t"
      "v_exp_f32 v53, v53\n\t"
      "v_exp_f32 v54, v54\n\t"
      "v_exp_f32 v55, v55\n\t"
      "v_mfma_f32_32x32x16_bf16 v[8:23], v[84:87], v[116:119], v[8:23]\n\t"
      "ds_read_b64 v[74:75], v215 offset:8\n\t"
      "v_exp_f32 v56, v56\n\t"
      "v_exp_f32 v57, v57\n\t"
      "v_exp_f32 v58, v58\n\t"
      "v_exp_f32 v59, v59\n\t"
      "v_smfmac_f32_32x32x32_bf16 v[168:183], v[96:99], v[48:55], v204\n\t"
      "ds_read_b64 v[80:81], v215 offset:16\n\t"
      "v_exp_f32 v60, v60\n\t"
      "v_exp_f32 v61, v61\n\t"
      "v_exp_f32 v62, v62\n\t"
      "v_exp_f32 v63, v63\n\t"
      "v_mfma_f32_32x32x16_bf16 v[24:39], v[76:79], v[120:123], 0\n\t"
      "s_waitcnt lgkmcnt(5)\n\t"
      "ds_read_b64 v[82:83], v215 offset:24\n\t"
      "v_exp_f32 v64, v64\n\t"
      "v_exp_f32 v65, v65\n\t"
      "v_exp_f32 v66, v66\n\t"
      "v_exp_f32 v67, v67\n\t"
      "v_smfmac_f32_32x32x32_bf16 v[136:151], v[92:95], v[56:63], v204\n\t"
      "v_exp_f32 v68, v68\n\t"
      "v_exp_f32 v69, v69\n\t"
      "v_exp_f32 v70, v70\n\t"
      "v_exp_f32 v71, v71\n\t"
      "v_mfma_f32_32x32x16_bf16 v[24:39], v[84:87], v[124:127], v[24:39]\n\t"
      "s_waitcnt lgkmcnt(4)\n\t"
      "v_exp_f32 v8, v8\n\t"
      "v_exp_f32 v9, v9\n\t"
      "v_exp_f32 v10, v10\n\t"
      "v_exp_f32 v11, v11\n\t"
      "v_smfmac_f32_32x32x32_bf16 v[136:151], v[100:103], v[64:71], v204\n\t"
      "s_waitcnt lgkmcnt(2)\n\t"
      "v_exp_f32 v12, v12\n\t"
      "v_exp_f32 v13, v13\n\t"
      "v_exp_f32 v14, v14\n\t"
      "v_exp_f32 v15, v15\n\t"
      "v_mfma_f32_32x32x16_bf16 v[40:55], v[72:75], v[104:107], 0\n\t"
      "v_exp_f32 v16, v16\n\t"
      "v_exp_f32 v17, v17\n\t"
      "v_exp_f32 v18, v18\n\t"
      "v_exp_f32 v19, v19\n\t"
      "v_smfmac_f32_32x32x32_bf16 v[152:167], v[92:95], v[8:15], v204\n\t"
      "s_waitcnt lgkmcnt(0)\n\t"
      "ds_read_b64_tr_b16 v[88:89], v216 offset:0\n\t"
      "v_exp_f32 v20, v20\n\t"
      "v_exp_f32 v21, v21\n\t"
      "v_exp_f32 v22, v22\n\t"
      "v_exp_f32 v23, v23\n\t"
      "s_setprio 0\n\t"
      "v_mfma_f32_32x32x16_bf16 v[40:55], v[80:83], v[108:111], v[40:55]\n\t"
      "ds_read_b64_tr_b16 v[90:91], v216 offset:224\n\t"
      "v_exp_f32 v24, v24\n\t"
      "v_exp_f32 v25, v25\n\t"
      "v_exp_f32 v26, v26\n\t"
      "v_exp_f32 v27, v27\n\t"
      "v_smfmac_f32_32x32x32_bf16 v[152:167], v[100:103], v[16:23], v204\n\t"
      "ds_read_b64_tr_b16 v[96:97], v216 offset:896\n\t"
      "v_exp_f32 v28, v28\n\t"
      "v_exp_f32 v29, v29\n\t"
      "v_exp_f32 v30, v30\n\t"
      "v_exp_f32 v31, v31\n\t"
      "v_mfma_f32_32x32x16_bf16 v[56:71], v[72:75], v[112:115], 0\n\t"
      "ds_read_b64_tr_b16 v[98:99], v216 offset:1120\n\t"
      "v_exp_f32 v32, v32\n\t"
      "v_exp_f32 v33, v33\n\t"
      "v_exp_f32 v34, v34\n\t"
      "v_exp_f32 v35, v35\n\t"
      "v_smfmac_f32_32x32x32_bf16 v[168:183], v[92:95], v[24:31], v204\n\t"
      "ds_read_b64 v[76:77], v215 offset:1792\n\t"
      "v_exp_f32 v36, v36\n\t"
      "v_exp_f32 v37, v37\n\t"
      "v_exp_f32 v38, v38\n\t"
      "v_exp_f32 v39, v39\n\t"
      "v_mfma_f32_32x32x16_bf16 v[56:71], v[80:83], v[116:119], v[56:71]\n\t"
      "ds_read_b64 v[78:79], v215 offset:1800\n\t"
      "v_exp_f32 v40, v40\n\t"
      "v_exp_f32 v41, v41\n\t"
      "v_exp_f32 v42, v42\n\t"
      "v_exp_f32 v43, v43\n\t"
      "v_smfmac_f32_32x32x32_bf16 v[168:183], v[100:103], v[32:39], v204\n\t"
      "ds_read_b64 v[84:85], v215 offset:1808\n\t"
      "v_exp_f32 v44, v44\n\t"
      "v_exp_f32 v45, v45\n\t"
      "v_exp_f32 v46, v46\n\t"
      "v_exp_f32 v47, v47\n\t"
      "v_mfma_f32_32x32x16_bf16 v[8:23], v[72:75], v[120:123], 0\n\t"
      "s_waitcnt lgkmcnt(5)\n\t"
      "ds_read_b64 v[86:87], v215 offset:1816\n\t"
      "v_exp_f32 v48, v48\n\t"
      "v_exp_f32 v49, v49\n\t"
      "v_exp_f32 v50, v50\n\t"
      "v_exp_f32 v51, v51\n\t"
      "v_smfmac_f32_32x32x32_bf16 v[136:151], v[88:91], v[40:47], v204\n\t"
      "v_exp_f32 v52, v52\n\t"
      "v_exp_f32 v53, v53\n\t"
      "v_exp_f32 v54, v54\n\t"
      "v_exp_f32 v55, v55\n\t"
      "v_mfma_f32_32x32x16_bf16 v[8:23], v[80:83], v[124:127], v[8:23]\n\t"
      "s_waitcnt lgkmcnt(4)\n\t"
      "v_exp_f32 v56, v56\n\t"
      "v_exp_f32 v57, v57\n\t"
      "v_exp_f32 v58, v58\n\t"
      "v_exp_f32 v59, v59\n\t"
      "v_smfmac_f32_32x32x32_bf16 v[136:151], v[96:99], v[48:55], v204\n\t"
      "s_waitcnt lgkmcnt(2)\n\t"
      "v_exp_f32 v60, v60\n\t"
      "v_exp_f32 v61, v61\n\t"
      "v_exp_f32 v62, v62\n\t"
      "v_exp_f32 v63, v63\n\t"
      "v_mfma_f32_32x32x16_bf16 v[24:39], v[76:79], v[104:107], 0\n\t"
      "v_exp_f32 v64, v64\n\t"
      "v_exp_f32 v65, v65\n\t"
      "v_exp_f32 v66, v66\n\t"
      "v_exp_f32 v67, v67\n\t"
      "v_smfmac_f32_32x32x32_bf16 v[152:167], v[88:91], v[56:63], v204\n\t"
      "s_waitcnt lgkmcnt(0)\n\t"
      "ds_read_b64_tr_b16 v[92:93], v216 offset:1792\n\t"
      "v_exp_f32 v68, v68\n\t"
      "v_exp_f32 v69, v69\n\t"
      "v_exp_f32 v70, v70\n\t"
      "v_exp_f32 v71, v71\n\t"
      "v_mfma_f32_32x32x16_bf16 v[24:39], v[84:87], v[108:111], v[24:39]\n\t"
      "ds_read_b64_tr_b16 v[94:95], v216 offset:2016\n\t"
      "v_exp_f32 v8, v8\n\t"
      "v_exp_f32 v9, v9\n\t"
      "v_exp_f32 v10, v10\n\t"
      "v_exp_f32 v11, v11\n\t"
      "v_smfmac_f32_32x32x32_bf16 v[152:167], v[96:99], v[64:71], v204\n\t"
      "ds_read_b64_tr_b16 v[100:101], v216 offset:2688\n\t"
      "v_exp_f32 v12, v12\n\t"
      "v_exp_f32 v13, v13\n\t"
      "v_exp_f32 v14, v14\n\t"
      "v_exp_f32 v15, v15\n\t"
      "v_mfma_f32_32x32x16_bf16 v[40:55], v[76:79], v[112:115], 0\n\t"
      "ds_read_b64_tr_b16 v[102:103], v216 offset:2912\n\t"
      "v_exp_f32 v16, v16\n\t"
      "v_exp_f32 v17, v17\n\t"
      "v_exp_f32 v18, v18\n\t"
      "v_exp_f32 v19, v19\n\t"
      "v_smfmac_f32_32x32x32_bf16 v[168:183], v[88:91], v[8:15], v204\n\t"
      "v_exp_f32 v20, v20\n\t"
      "v_exp_f32 v21, v21\n\t"
      "v_exp_f32 v22, v22\n\t"
      "v_exp_f32 v23, v23\n\t"
      "v_mfma_f32_32x32x16_bf16 v[40:55], v[84:87], v[116:119], v[40:55]\n\t"
      "v_exp_f32 v24, v24\n\t"
      "v_exp_f32 v25, v25\n\t"
      "v_exp_f32 v26, v26\n\t"
      "v_exp_f32 v27, v27\n\t"
      "v_smfmac_f32_32x32x32_bf16 v[168:183], v[96:99], v[16:23], v204\n\t"
      "v_exp_f32 v28, v28\n\t"
      "v_exp_f32 v29, v29\n\t"
      "v_exp_f32 v30, v30\n\t"
      "v_exp_f32 v31, v31\n\t"
      "v_mfma_f32_32x32x16_bf16 v[56:71], v[76:79], v[120:123], 0\n\t"
      "s_waitcnt lgkmcnt(2)\n\t"
      "v_exp_f32 v32, v32\n\t"
      "v_exp_f32 v33, v33\n\t"
      "v_exp_f32 v34, v34\n\t"
      "v_exp_f32 v35, v35\n\t"
      "v_smfmac_f32_32x32x32_bf16 v[136:151], v[92:95], v[24:31], v204\n\t"
      "v_exp_f32 v36, v36\n\t"
      "v_exp_f32 v37, v37\n\t"
      "v_exp_f32 v38, v38\n\t"
      "v_exp_f32 v39, v39\n\t"
      "v_mfma_f32_32x32x16_bf16 v[56:71], v[84:87], v[124:127], v[56:71]\n\t"
      "s_waitcnt lgkmcnt(0)\n\t"
      "v_exp_f32 v40, v40\n\t"
      "v_exp_f32 v41, v41\n\t"
      "v_exp_f32 v42, v42\n\t"
      "v_exp_f32 v43, v43\n\t"
      "v_smfmac_f32_32x32x32_bf16 v[136:151], v[100:103], v[32:39], v204\n\t"
      "v_exp_f32 v44, v44\n\t"
      "v_exp_f32 v45, v45\n\t"
      "v_exp_f32 v46, v46\n\t"
      "v_exp_f32 v47, v47\n\t"
      "v_mfma_f32_32x32x16_bf16 v[8:23], v[72:75], v[128:131], 0\n\t"
      "ds_read_b64 v[72:73], v200 offset:0\n\t"
      "v_exp_f32 v48, v48\n\t"
      "v_exp_f32 v49, v49\n\t"
      "v_exp_f32 v50, v50\n\t"
      "v_exp_f32 v51, v51\n\t"
      "v_smfmac_f32_32x32x32_bf16 v[152:167], v[92:95], v[40:47], v204\n\t"
      "ds_read_b64 v[74:75], v200 offset:8\n\t"
      "v_exp_f32 v52, v52\n\t"
      "v_exp_f32 v53, v53\n\t"
      "v_exp_f32 v54, v54\n\t"
      "v_exp_f32 v55, v55\n\t"
      "v_mfma_f32_32x32x16_bf16 v[8:23], v[80:83], v[132:135], v[8:23]\n\t"
      "ds_read_b64 v[80:81], v200 offset:16\n\t"
      "ds_write_b128 v210, v[136:139] offset:0\n\t"
      "v_exp_f32 v56, v56\n\t"
      "v_exp_f32 v57, v57\n\t"
      "v_exp_f32 v58, v58\n\t"
      "v_exp_f32 v59, v59\n\t"
      "v_smfmac_f32_32x32x32_bf16 v[152:167], v[100:103], v[48:55], v204\n\t"
      "ds_read_b64 v[82:83], v200 offset:24\n\t"
      "ds_write_b128 v210, v[140:143] offset:1024\n\t"
      "v_exp_f32 v60, v60\n\t"
      "v_exp_f32 v61, v61\n\t"
      "v_exp_f32 v62, v62\n\t"
      "v_exp_f32 v63, v63\n\t"
      "v_mfma_f32_32x32x16_bf16 v[24:39], v[76:79], v[128:131], 0\n\t"
      "ds_read_b64 v[76:77], v200 offset:1792\n\t"
      "ds_write_b128 v210, v[144:147] offset:2048\n\t"
      "v_exp_f32 v64, v64\n\t"
      "v_exp_f32 v65, v65\n\t"
      "v_exp_f32 v66, v66\n\t"
      "v_exp_f32 v67, v67\n\t"
      "v_smfmac_f32_32x32x32_bf16 v[168:183], v[92:95], v[56:63], v204\n\t"
      "ds_read_b64 v[78:79], v200 offset:1800\n\t"
      "ds_write_b128 v210, v[148:151] offset:3072\n\t"
      "v_exp_f32 v68, v68\n\t"
      "v_exp_f32 v69, v69\n\t"
      "v_exp_f32 v70, v70\n\t"
      "v_exp_f32 v71, v71\n\t"
      "v_mfma_f32_32x32x16_bf16 v[24:39], v[84:87], v[132:135], v[24:39]\n\t"
      "ds_read_b64 v[84:85], v200 offset:1808\n\t"
      "ds_write_b128 v211, v[152:155] offset:0\n\t"
      "v_exp_f32 v8, v8\n\t"
      "v_exp_f32 v9, v9\n\t"
      "v_exp_f32 v10, v10\n\t"
      "v_exp_f32 v11, v11\n\t"
      "v_smfmac_f32_32x32x32_bf16 v[168:183], v[100:103], v[64:71], v204\n\t"
      "s_waitcnt lgkmcnt(10)\n\t"
      "ds_read_b64 v[86:87], v200 offset:1816\n\t"
      "ds_write_b128 v211, v[156:159] offset:1024\n\t"
      "v_exp_f32 v12, v12\n\t"
      "v_exp_f32 v13, v13\n\t"
      "v_exp_f32 v14, v14\n\t"
      "v_exp_f32 v15, v15\n\t"
      "v_mfma_f32_32x32x16_bf16 v[40:55], v[72:75], v[128:131], 0\n\t"
      "ds_write_b128 v211, v[160:163] offset:2048\n\t"
      "v_exp_f32 v16, v16\n\t"
      "v_exp_f32 v17, v17\n\t"
      "v_exp_f32 v18, v18\n\t"
      "v_exp_f32 v19, v19\n\t"
      "v_smfmac_f32_32x32x32_bf16 v[184:199], v[88:91], v[8:15], v204\n\t"
      "s_waitcnt lgkmcnt(10)\n\t"
      "ds_read_b64_tr_b16 v[88:89], v201 offset:0\n\t"
      "ds_write_b128 v211, v[164:167] offset:3072\n\t"
      "v_exp_f32 v20, v20\n\t"
      "v_exp_f32 v21, v21\n\t"
      "v_exp_f32 v22, v22\n\t"
      "v_exp_f32 v23, v23\n\t"
      "v_mfma_f32_32x32x16_bf16 v[40:55], v[80:83], v[132:135], v[40:55]\n\t"
      "ds_read_b64_tr_b16 v[90:91], v201 offset:224\n\t"
      "ds_write_b128 v212, v[168:171] offset:0\n\t"
      "v_exp_f32 v24, v24\n\t"
      "v_exp_f32 v25, v25\n\t"
      "v_exp_f32 v26, v26\n\t"
      "v_exp_f32 v27, v27\n\t"
      "v_smfmac_f32_32x32x32_bf16 v[184:199], v[96:99], v[16:23], v204\n\t"
      "s_waitcnt lgkmcnt(10)\n\t"
      "ds_read_b64_tr_b16 v[96:97], v201 offset:896\n\t"
      "ds_write_b128 v212, v[172:175] offset:1024\n\t"
      "v_exp_f32 v28, v28\n\t"
      "v_exp_f32 v29, v29\n\t"
      "v_exp_f32 v30, v30\n\t"
      "v_exp_f32 v31, v31\n\t"
      "v_mfma_f32_32x32x16_bf16 v[56:71], v[76:79], v[128:131], 0\n\t"
      "ds_read_b64_tr_b16 v[98:99], v201 offset:1120\n\t"
      "ds_write_b128 v212, v[176:179] offset:2048\n\t"
      "v_exp_f32 v32, v32\n\t"
      "v_exp_f32 v33, v33\n\t"
      "v_exp_f32 v34, v34\n\t"
      "v_exp_f32 v35, v35\n\t"
      "v_smfmac_f32_32x32x32_bf16 v[184:199], v[92:95], v[24:31], v204\n\t"
      "s_waitcnt lgkmcnt(10)\n\t"
      "ds_read_b64_tr_b16 v[92:93], v201 offset:1792\n\t"
      "ds_write_b128 v212, v[180:183] offset:3072\n\t"
      "v_exp_f32 v36, v36\n\t"
      "v_exp_f32 v37, v37\n\t"
      "v_exp_f32 v38, v38\n\t"
      "v_exp_f32 v39, v39\n\t"
      "v_mfma_f32_32x32x16_bf16 v[56:71], v[84:87], v[132:135], v[56:71]\n\t"
      "ds_read_b64_tr_b16 v[94:95], v201 offset:2016\n\t"
      "v_exp_f32 v40, v40\n\t"
      "v_exp_f32 v41, v41\n\t"
      "v_exp_f32 v42, v42\n\t"
      "v_exp_f32 v43, v43\n\t"
      "s_waitcnt lgkmcnt(0)\n\t"
      "s_barrier\n\t"
      "v_smfmac_f32_32x32x32_bf16 v[184:199], v[100:103], v[32:39], v204\n\t"
      "ds_read_b64_tr_b16 v[100:101], v201 offset:2688\n\t"
      "ds_read_b128 v[136:139], v206 offset:0\n\t"
      "ds_read_b128 v[140:143], v206 offset:1024\n\t"
      "v_exp_f32 v44, v44\n\t"
      "v_exp_f32 v45, v45\n\t"
      "v_exp_f32 v46, v46\n\t"
      "v_exp_f32 v47, v47\n\t"
      "s_waitcnt lgkmcnt(11)\n\t"
      "ds_read_b64_tr_b16 v[102:103], v201 offset:2912\n\t"
      "ds_read_b128 v[144:147], v206 offset:2048\n\t"
      "ds_read_b128 v[148:151], v206 offset:3072\n\t"
      "v_exp_f32 v48, v48\n\t"
      "v_exp_f32 v49, v49\n\t"
      "v_exp_f32 v50, v50\n\t"
      "v_exp_f32 v51, v51\n\t"
      "v_smfmac_f32_32x32x32_bf16 v[184:199], v[88:91], v[40:47], v204\n\t"
      "ds_read_b128 v[152:155], v207 offset:0\n\t"
      "ds_read_b128 v[156:159], v207 offset:1024\n\t"
      "v_exp_f32 v52, v52\n\t"
      "v_exp_f32 v53, v53\n\t"
      "v_exp_f32 v54, v54\n\t"
      "v_exp_f32 v55, v55\n\t"
      "s_waitcnt lgkmcnt(12)\n\t"
      "ds_read_b128 v[160:163], v207 offset:2048\n\t"
      "ds_read_b128 v[164:167], v207 offset:3072\n\t"
      "v_exp_f32 v56, v56\n\t"
      "v_exp_f32 v57, v57\n\t"
      "v_exp_f32 v58, v58\n\t"
      "v_exp_f32 v59, v59\n\t"
      "v_smfmac_f32_32x32x32_bf16 v[184:199], v[96:99], v[48:55], v204\n\t"
      "ds_read_b128 v[168:171], v208 offset:0\n\t"
      "ds_read_b128 v[172:175], v208 offset:1024\n\t"
      "v_exp_f32 v60, v60\n\t"
      "v_exp_f32 v61, v61\n\t"
      "v_exp_f32 v62, v62\n\t"
      "v_exp_f32 v63, v63\n\t"
      "s_waitcnt lgkmcnt(5)\n\t"
      "v_pk_add_f32 v[136:137], v[136:137], v[152:153]\n\t"
      "v_pk_add_f32 v[138:139], v[138:139], v[154:155]\n\t"
      "s_waitcnt lgkmcnt(4)\n\t"
      "v_pk_add_f32 v[140:141], v[140:141], v[156:157]\n\t"
      "v_pk_add_f32 v[142:143], v[142:143], v[158:159]\n\t"
      "s_waitcnt lgkmcnt(3)\n\t"
      "v_pk_add_f32 v[144:145], v[144:145], v[160:161]\n\t"
      "v_pk_add_f32 v[146:147], v[146:147], v[162:163]\n\t"
      "s_waitcnt lgkmcnt(12)\n\t"
      "ds_read_b128 v[176:179], v208 offset:2048\n\t"
      "ds_read_b128 v[180:183], v208 offset:3072\n\t"
      "v_exp_f32 v64, v64\n\t"
      "v_exp_f32 v65, v65\n\t"
      "v_exp_f32 v66, v66\n\t"
      "v_exp_f32 v67, v67\n\t"
      "s_waitcnt lgkmcnt(4)\n\t"
      "v_pk_add_f32 v[148:149], v[148:149], v[164:165]\n\t"
      "v_pk_add_f32 v[150:151], v[150:151], v[166:167]\n\t"
      "s_waitcnt lgkmcnt(3)\n\t"
      "v_pk_add_f32 v[136:137], v[136:137], v[168:169]\n\t"
      "v_pk_add_f32 v[138:139], v[138:139], v[170:171]\n\t"
      "s_waitcnt lgkmcnt(2)\n\t"
      "v_pk_add_f32 v[140:141], v[140:141], v[172:173]\n\t"
      "v_pk_add_f32 v[142:143], v[142:143], v[174:175]\n\t"
      "v_smfmac_f32_32x32x32_bf16 v[184:199], v[92:95], v[56:63], v204\n\t"
      "v_exp_f32 v68, v68\n\t"
      "v_exp_f32 v69, v69\n\t"
      "v_exp_f32 v70, v70\n\t"
      "v_exp_f32 v71, v71\n\t"
      "s_waitcnt lgkmcnt(1)\n\t"
      "v_pk_add_f32 v[144:145], v[144:145], v[176:177]\n\t"
      "v_pk_add_f32 v[146:147], v[146:147], v[178:179]\n\t"
      "s_waitcnt lgkmcnt(0)\n\t"
      "v_pk_add_f32 v[148:149], v[148:149], v[180:181]\n\t"
      "v_pk_add_f32 v[150:151], v[150:151], v[182:183]\n\t"
      "s_waitcnt lgkmcnt(10)\n\t"
      "s_nop 0\n\t"
      "v_smfmac_f32_32x32x32_bf16 v[184:199], v[100:103], v[64:71], v204\n\t"
      "s_nop 7\n\t"
      "s_nop 3\n\t"
      "v_pk_add_f32 v[196:197], v[196:197], v[148:149]\n\t"
      "v_pk_add_f32 v[198:199], v[198:199], v[150:151]\n\t"
      "v_pk_add_f32 v[184:185], v[184:185], v[136:137]\n\t"
      "v_pk_add_f32 v[186:187], v[186:187], v[138:139]\n\t"
      "v_pk_add_f32 v[188:189], v[188:189], v[140:141]\n\t"
      "v_pk_add_f32 v[190:191], v[190:191], v[142:143]\n\t"
      "v_pk_add_f32 v[192:193], v[192:193], v[144:145]\n\t"
      "v_pk_add_f32 v[194:195], v[194:195], v[146:147]\n\t"
      "v_mov_b32 v40, v196\n\t"
      "v_mov_b32 v41, v196\n\t"
      "v_pk_add_f32 v[42:43], v[184:185], v[186:187]\n\t"
      "v_pk_add_f32 v[44:45], v[188:189], v[190:191]\n\t"
      "v_permlane32_swap_b32 v40, v41\n\t"
      "v_pk_add_f32 v[46:47], v[192:193], v[194:195]\n\t"
      "v_rcp_f32 v40, v41\n\t"
      "v_pk_add_f32 v[48:49], v[196:197], v[198:199]\n\t"
      "v_mul_f32 v184, v184, v40\n\t"
      "v_mul_f32 v185, v185, v40\n\t"
      "v_mul_f32 v186, v186, v40\n\t"
      "v_mul_f32 v187, v187, v40\n\t"
      "v_cvt_pk_bf16_f32 v8, v184, v185\n\t"
      "v_cvt_pk_bf16_f32 v9, v186, v187\n\t"
      "ds_write_b64 v209, v[8:9] offset:0\n\t"
      "v_mul_f32 v188, v188, v40\n\t"
      "v_mul_f32 v189, v189, v40\n\t"
      "v_mul_f32 v190, v190, v40\n\t"
      "v_mul_f32 v191, v191, v40\n\t"
      "v_cvt_pk_bf16_f32 v10, v188, v189\n\t"
      "v_cvt_pk_bf16_f32 v11, v190, v191\n\t"
      "ds_write_b64 v209, v[10:11] offset:16\n\t"
      "v_mul_f32 v192, v192, v40\n\t"
      "v_mul_f32 v193, v193, v40\n\t"
      "v_mul_f32 v194, v194, v40\n\t"
      "v_mul_f32 v195, v195, v40\n\t"
      "v_cvt_pk_bf16_f32 v12, v192, v193\n\t"
      "v_cvt_pk_bf16_f32 v13, v194, v195\n\t"
      "ds_write_b64 v209, v[12:13] offset:32\n\t"
      "v_mul_f32 v196, v196, v40\n\t"
      "v_mul_f32 v197, v197, v40\n\t"
      "v_mul_f32 v198, v198, v40\n\t"
      "v_mul_f32 v199, v199, v40\n\t"
      "v_cvt_pk_bf16_f32 v14, v196, v197\n\t"
      "v_cvt_pk_bf16_f32 v15, v198, v199\n\t"
      "s_mov_b32 exec_hi, 0\n\t"
      "ds_write_b64 v209, v[14:15] offset:48\n\t"
      "s_mov_b64 exec, -1\n\t"
      "ds_read_b128 v[16:19], v206\n\t"
      "ds_read_b128 v[20:23], v206 offset:1024\n\t"
      "v_pk_add_f32 v[42:43], v[42:43], v[44:45]\n\t"
      "v_pk_add_f32 v[46:47], v[46:47], v[48:49]\n\t"
      "v_cmp_nle_f32 vcc, 0x3b800000, v41\n\t"
      "v_pk_add_f32 v[42:43], v[42:43], v[46:47]\n\t"
      "s_mov_b32 s65, 0x71800000\n\t"
      "v_cmp_nge_f32 s[66:67], s65, v41\n\t"
      "v_add_f32 v42, v42, v43\n\t"
      "s_waitcnt lgkmcnt(1)\n\t"
      "global_store_dwordx4 v203, v[16:19], %[op] sc0 sc1\n\t"
      "s_mov_b32 exec_hi, 0xffff\n\t"
      "s_waitcnt lgkmcnt(0)\n\t"
      "global_store_dwordx4 v203, v[20:23], %[op] offset:1024 sc0 sc1\n\t"
      "s_mov_b64 exec, -1\n\t"
      "s_nop 1\n\t"
      "s_or_b64 s[66:67], s[66:67], vcc\n\t"
      "s_movk_i32 s65, 0x1f8\n\t"
      "v_cmp_class_f32 vcc, v42, s65\n\t"
      "s_nop 1\n\t"
      "s_orn2_b64 s[66:67], s[66:67], vcc\n\t"
      "s_cmp_lg_u64 s[66:67], 0\n\t"
      "s_cselect_b32 %[flag], 1, 0\n\t"
      "s_mov_b32 m0, s68\n\t"
      : [flag] "=&s"(flag)
      : [tid] "v"(tid), [qp] "s"(qp), [op] "s"(op), [glo] "s"(glo), [ghi] "s"(ghi), [m0b] "s"(m0b), [cs] "s"(cs),
        [kqoff] "s"(kqoff), [vvoff] "s"(vvoff), [qsto] "s"(qsto), [qgo] "s"(qgo), [kq] "s"(kq), [qg] "s"(qg), [w] "s"(w), [xoff] "s"(xoff)
      : "v8", "v9", "v10", "v11", "v12", "v13", "v14", "v15", "v16", "v17", "v18", "v19", "v20", "v21", "v22", "v23", "v24", "v25", "v26", "v27", "v28", "v29", "v30", "v31", "v32", "v33", "v34", "v35", "v36", "v37", "v38", "v39", "v40", "v41", "v42", "v43", "v44", "v45", "v46", "v47", "v48", "v49", "v50", "v51", "v52", "v53", "v54", "v55", "v56", "v57", "v58", "v59", "v60", "v61", "v62", "v63", "v64", "v65", "v66", "v67", "v68", "v69", "v70", "v71", "v72", "v73", "v74", "v75", "v76", "v77", "v78", "v79", "v80", "v81", "v82", "v83", "v84", "v85", "v86", "v87", "v88", "v89", "v90", "v91", "v92", "v93", "v94", "v95", "v96", "v97", "v98", "v99", "v100", "v101", "v102", "v103", "v104", "v105", "v106", "v107", "v108", "v109", "v110", "v111", "v112", "v113", "v114", "v115", "v116", "v117", "v118", "v119", "v120", "v121", "v122", "v123", "v124", "v125", "v126", "v127", "v128", "v129", "v130", "v131", "v132", "v133", "v134", "v135", "v136", "v137", "v138", "v139", "v140", "v141", "v142", "v143", "v144", "v145", "v146", "v147", "v148", "v149", "v150", "v151", "v152", "v153", "v154", "v155", "v156", "v157", "v158", "v159", "v160", "v161", "v162", "v163", "v164", "v165", "v166", "v167", "v168", "v169", "v170", "v171", "v172", "v173", "v174", "v175", "v176", "v177", "v178", "v179", "v180", "v181", "v182", "v183", "v184", "v185", "v186", "v187", "v188", "v189", "v190", "v191", "v192", "v193", "v194", "v195", "v196", "v197", "v198", "v199", "v200", "v201", "v202", "v203", "v204", "v205", "v206", "v207", "v208", "v209", "v210", "v211", "v212", "v213", "v214", "v215", "v216", "s60", "s61", "s62", "s63", "s64", "s65", "s66", "s67", "s68", "s69", "s70", "s71", "scc", "vcc", "memory");
  if (__builtin_expect(flag, 0)) {
    asm volatile("s_waitcnt vmcnt(0)" ::: "memory");
    safe_rows(q + hb + (size_t)row0 * D, k + hb, v + hb, op, tid & 63);
  }
}

extern "C" void kernel_launch(void* const* d_in, const int* in_sizes, int n_in, void* d_out, int out_size, void* d_ws, size_t ws_size, hipStream_t stream) {
  attn_fused<<<256, NW * 64, 0, stream>>>((const uint16_t*)d_in[0], (const uint16_t*)d_in[1], (const uint16_t*)d_in[2], (uint16_t*)d_out);
}
```
